```python
import torch

def _softmax(x, axis=-1):
    return torch.softmax(x, dim=axis)
def _gelu(x):
    return torch.nn.functional.gelu(x, approximate="tanh")
def _silu(x):
    return torch.nn.functional.silu(x)
def _sigmoid(x):
    return torch.sigmoid(x)
def _layernorm(x, axis=-1, eps=1e-5):
    m = x.mean(dim=axis, keepdim=True)
    v = x.var(dim=axis, keepdim=True, unbiased=False)
    return (x - m) / torch.sqrt(v + eps)
def _scatter_add(tgt, idx, src, axis=0):
    return tgt.clone().index_add_(axis, idx, src)
def _segment_sum(src, seg, n):
    out = torch.zeros((n,) + src.shape[1:], dtype=src.dtype)
    return out.index_add_(0, seg, src)
def _pad(x, pw):
    flat = tuple(v for pair in reversed(pw) for v in pair)
    return torch.nn.functional.pad(x, flat)

kernel_name = 'sequential_preLN__mha_block_cached__swiglu_ffn__decode'

def setup_inputs():

    _w = (lambda a: (lambda t: t.to(torch.bfloat16) if t.is_floating_point() else t)(torch.as_tensor(a)))
    def _A_setup():
        B, Sq, D = 8, 1, 640
        H, Sk, Dh = 32, 4096, 128
        return {
            "x": _w((lambda *s: torch.randn(*s, dtype=torch.bfloat16) * 0.05)(B, Sq, D)),
            "k_cache": _w((lambda *s: torch.randn(*s, dtype=torch.bfloat16) * 3.2)(B, H, Sk, Dh)),
            "v_cache": _w((lambda *s: torch.randn(*s, dtype=torch.bfloat16) * 0.4)(B, H, Sk, Dh)),
            "wq": _w((lambda *s: torch.randn(*s, dtype=torch.bfloat16) * 3.2)(D, H * Dh) * (D ** -0.5)),
            "wo": _w((lambda *s: torch.randn(*s, dtype=torch.bfloat16) * 0.4)(H * Dh, D) * ((H * Dh) ** -0.5)),
        }
    _A_in = _A_setup()

    def _B_setup():
        B, S, D, F = 8, 1, 640, 1728
        return {
            "x": _w((lambda *s: torch.randn(*s, dtype=torch.bfloat16) * 0.05)(B, S, D)),
            "w_gate": _w((lambda *s: torch.randn(*s, dtype=torch.bfloat16) * 0.05)(D, F)),
            "w_up": _w((lambda *s: torch.randn(*s, dtype=torch.bfloat16) * 0.05)(D, F)),
            "w_down": _w((lambda *s: torch.randn(*s, dtype=torch.bfloat16) * 0.05)(F, D)),
        }
    _B_in = _B_setup()

    return {
        "A_x": _A_in["x"],
        "A_k_cache": _A_in["k_cache"],
        "A_v_cache": _A_in["v_cache"],
        "A_wq": _A_in["wq"],
        "A_wo": _A_in["wo"],
        "B_x": _B_in["x"],
        "B_w_gate": _B_in["w_gate"],
        "B_w_up": _B_in["w_up"],
        "B_w_down": _B_in["w_down"],
    }

def reference(A_x, A_k_cache, A_v_cache, A_wq, A_wo, B_x, B_w_gate, B_w_up, B_w_down):
    def _A_ref(x, k_cache, v_cache, wq, wo):
        B, Sq, D = x.shape
        H, Dh = 32, 128
        q = (lambda x, *d: x.permute(*d))((x @ wq).reshape(B, Sq, H, Dh), 0, 2, 1, 3)
        scores = torch.einsum("bhqd,bhkd->bhqk", q, k_cache) * (Dh ** -0.5)
        p = _softmax(scores, axis=-1)
        o = torch.einsum("bhqk,bhkd->bhqd", p, v_cache)
        return (lambda x, *d: x.permute(*d))(o, 0, 2, 1, 3).reshape(B, Sq, H * Dh) @ wo

    def _B_ref(x, w_gate, w_up, w_down):
        g = _silu(x @ w_gate)
        u = x @ w_up
        return (g * u) @ w_down

    x = A_x
    h = x + _A_ref(_layernorm(x, axis=-1), A_k_cache, A_v_cache, A_wq, A_wo)
    h2 = h + _B_ref(_layernorm(h, axis=-1), B_w_gate, B_w_up, B_w_down)
    return h2
```

```cpp
#include <hip/hip_runtime.h>
#include <hip/hip_bf16.h>
#include <stdint.h>

#define MIMIC 0

typedef unsigned u32x4 __attribute__((ext_vector_type(4)));
typedef float f32x4 __attribute__((ext_vector_type(4)));
typedef short bf16x8 __attribute__((ext_vector_type(8)));
typedef __bf16 bf16x2_t __attribute__((ext_vector_type(2)));
typedef __bf16 bf16x8_t __attribute__((ext_vector_type(8)));

constexpr int B = 8, D = 640, H = 32, SK = 4096, DH = 128, F = 1728;

__device__ __forceinline__ float bf2f(unsigned short u) { return __uint_as_float(((unsigned)u) << 16); }
__device__ __forceinline__ unsigned short f2bf(float f) { __bf16 b = (__bf16)f; return __builtin_bit_cast(unsigned short, b); }
__device__ __forceinline__ float rbf(float f) {
  return f;
}
__device__ __forceinline__ float wave_sum(float v) {
  for (int o = 32; o > 0; o >>= 1) v += __shfl_xor(v, o);
  return v;
}
__device__ __forceinline__ float wave_max(float v) {
  for (int o = 32; o > 0; o >>= 1) v = fmaxf(v, __shfl_xor(v, o));
  return v;
}

__device__ void ln640(const float* in, float* out, int tid, int nthr) {
  int lane = tid & 63;
  float s = 0.f;
  for (int i = lane; i < D; i += 64) s += in[i];
  s = wave_sum(s);
  float mean = s / D;
  float v = 0.f;
  for (int i = lane; i < D; i += 64) { float d = in[i] - mean; v += d * d; }
  v = wave_sum(v) / D;
  float mb = rbf(mean), vb = rbf(v);
  float den = rbf(sqrtf(rbf(vb + 1e-5f)));
  for (int i = tid; i < D; i += nthr) out[i] = rbf(rbf(in[i] - mb) / den);
}

__global__ void __launch_bounds__(512) k_attn(const unsigned short* __restrict__ x, const unsigned short* __restrict__ kc,
                                              const unsigned short* __restrict__ vc, const unsigned short* __restrict__ wq,
                                              float* __restrict__ o_out) {
  __shared__ float xs[D], xn[D], red[8 * 128], qs[128], sc[SK], wm[8], wsum[8];
  const int bh = blockIdx.x, b = bh >> 5, h = bh & 31;
  const int tid = threadIdx.x, lane = tid & 63, w = tid >> 6, g = lane >> 4, c = lane & 15;
  for (int i = tid; i < D; i += 512) xs[i] = bf2f(x[b * D + i]);
  __syncthreads();
  ln640(xs, xn, tid, 512);
  __syncthreads();
  {
    float acc[8] = {0, 0, 0, 0, 0, 0, 0, 0};
    for (int i = 0; i < 20; ++i) {
      int r = w * 80 + 4 * i + g;
      u32x4 wv = *(const u32x4*)(wq + (size_t)r * (H * DH) + h * DH + 8 * c);
      float xv = xn[r];
      for (int k = 0; k < 4; ++k) {
        acc[2 * k] += xv * __uint_as_float(wv[k] << 16);
        acc[2 * k + 1] += xv * __uint_as_float(wv[k] & 0xffff0000u);
      }
    }
    for (int k = 0; k < 8; ++k) { acc[k] += __shfl_xor(acc[k], 16); acc[k] += __shfl_xor(acc[k], 32); }
    if (g == 0) for (int k = 0; k < 8; ++k) red[w * 128 + 8 * c + k] = acc[k];
    __syncthreads();
    if (tid < 128) { float s = 0; for (int j = 0; j < 8; ++j) s += red[j * 128 + tid]; qs[tid] = rbf(s); }
    __syncthreads();
  }
  float q[8];
  for (int k = 0; k < 8; ++k) q[k] = qs[8 * c + k];
  const unsigned short* kb = kc + (size_t)bh * SK * DH;
  const unsigned short* vb = vc + (size_t)bh * SK * DH;
  for (int i = 0; i < 128; ++i) {
    int key = w * 512 + 4 * i + g;
    u32x4 kv = __builtin_nontemporal_load((const u32x4*)(kb + (size_t)key * DH + 8 * c));
    float d = 0.f;
    for (int k = 0; k < 4; ++k) { d += q[2 * k] * __uint_as_float(kv[k] << 16); d += q[2 * k + 1] * __uint_as_float(kv[k] & 0xffff0000u); }
    d += __shfl_xor(d, 1); d += __shfl_xor(d, 2); d += __shfl_xor(d, 4); d += __shfl_xor(d, 8);
    if (c == 0) sc[key] = rbf(rbf(d) * 0.08838834764831845f);
  }
  __syncthreads();
  float m = -INFINITY;
  for (int i = tid; i < SK; i += 512) m = fmaxf(m, sc[i]);
  m = wave_max(m);
  if (lane == 0) wm[w] = m;
  __syncthreads();
  m = wm[0];
  for (int j = 1; j < 8; ++j) m = fmaxf(m, wm[j]);
  float s = 0.f;
  for (int i = tid; i < SK; i += 512) { float e = __expf(sc[i] - m); sc[i] = e; s += e; }
  s = wave_sum(s);
  if (lane == 0) wsum[w] = s;
  __syncthreads();
  s = 0.f;
  for (int j = 0; j < 8; ++j) s += wsum[j];
  float inv = 1.f / s;
  float acc[8] = {0, 0, 0, 0, 0, 0, 0, 0};
  for (int i = 0; i < 128; ++i) {
    int key = w * 512 + 4 * i + g;
    u32x4 vv = __builtin_nontemporal_load((const u32x4*)(vb + (size_t)key * DH + 8 * c));
    float p = rbf(sc[key] * inv);
    for (int k = 0; k < 4; ++k) {
      acc[2 * k] += p * __uint_as_float(vv[k] << 16);
      acc[2 * k + 1] += p * __uint_as_float(vv[k] & 0xffff0000u);
    }
  }
  for (int k = 0; k < 8; ++k) { acc[k] += __shfl_xor(acc[k], 16); acc[k] += __shfl_xor(acc[k], 32); }
  if (g == 0) for (int k = 0; k < 8; ++k) red[w * 128 + 8 * c + k] = acc[k];
  __syncthreads();
  if (tid < 128) { float t = 0; for (int j = 0; j < 8; ++j) t += red[j * 128 + tid]; o_out[bh * 128 + tid] = rbf(t); }
}

__global__ void __launch_bounds__(256) k_wo(const unsigned short* __restrict__ x, const float* __restrict__ o,
                                            const unsigned short* __restrict__ wo, float* __restrict__ hbuf) {
  __shared__ float red[256][65];
  const int c0 = blockIdx.x * 8, tid = threadIdx.x;
  float acc[8][8];
  for (int b = 0; b < 8; ++b) for (int k = 0; k < 8; ++k) acc[b][k] = 0.f;
  for (int r = tid; r < H * DH; r += 256) {
    u32x4 wv = *(const u32x4*)(wo + (size_t)r * D + c0);
    float wf[8];
    for (int k = 0; k < 4; ++k) { wf[2 * k] = __uint_as_float(wv[k] << 16); wf[2 * k + 1] = __uint_as_float(wv[k] & 0xffff0000u); }
    for (int b = 0; b < 8; ++b) { float ov = o[b * 4096 + r]; for (int k = 0; k < 8; ++k) acc[b][k] += ov * wf[k]; }
  }
  for (int b = 0; b < 8; ++b) for (int k = 0; k < 8; ++k) red[tid][b * 8 + k] = acc[b][k];
  __syncthreads();
  if (tid < 64) {
    float s = 0.f;
    for (int j = 0; j < 256; ++j) s += red[j][tid];
    int b = tid >> 3, k = tid & 7;
    hbuf[b * D + c0 + k] = rbf(bf2f(x[b * D + c0 + k]) + rbf(s));
  }
}

__global__ void __launch_bounds__(256) k_gu(const float* __restrict__ hbuf, const unsigned short* __restrict__ wg,
                                            const unsigned short* __restrict__ wu, float* __restrict__ act) {
  __shared__ float hs[8][D], hn[8][D];
  __shared__ float red[128][129];
  const int c0 = blockIdx.x * 8, tid = threadIdx.x;
  for (int i = tid; i < 8 * D; i += 256) hs[i / D][i % D] = hbuf[i];
  __syncthreads();
  for (int b = 0; b < 8; ++b) ln640(hs[b], hn[b], tid, 256);
  __syncthreads();
  const int half = tid >> 7, t = tid & 127;
  const unsigned short* W = half ? wu : wg;
  float acc[8][8];
  for (int b = 0; b < 8; ++b) for (int k = 0; k < 8; ++k) acc[b][k] = 0.f;
  for (int r = t; r < D; r += 128) {
    u32x4 wv = *(const u32x4*)(W + (size_t)r * F + c0);
    float wf[8];
    for (int k = 0; k < 4; ++k) { wf[2 * k] = __uint_as_float(wv[k] << 16); wf[2 * k + 1] = __uint_as_float(wv[k] & 0xffff0000u); }
    for (int b = 0; b < 8; ++b) { float hv = hn[b][r]; for (int k = 0; k < 8; ++k) acc[b][k] += hv * wf[k]; }
  }
  for (int b = 0; b < 8; ++b) for (int k = 0; k < 8; ++k) red[t][half * 64 + b * 8 + k] = acc[b][k];
  __syncthreads();
  if (tid < 64) {
    float sg = 0.f, su = 0.f;
    for (int j = 0; j < 128; ++j) { sg += red[j][tid]; su += red[j][64 + tid]; }
    sg = rbf(sg); su = rbf(su);
    float gl = rbf(sg / (1.f + __expf(-sg)));
    int b = tid >> 3, k = tid & 7;
    act[b * F + c0 + k] = rbf(gl * su);
  }
}

__global__ void __launch_bounds__(256) k_down(const float* __restrict__ hbuf, const float* __restrict__ act,
                                              const unsigned short* __restrict__ wd, unsigned short* __restrict__ out) {
  __shared__ float red[256][65];
  const int c0 = blockIdx.x * 8, tid = threadIdx.x;
  float acc[8][8];
  for (int b = 0; b < 8; ++b) for (int k = 0; k < 8; ++k) acc[b][k] = 0.f;
  for (int r = tid; r < F; r += 256) {
    u32x4 wv = *(const u32x4*)(wd + (size_t)r * D + c0);
    float wf[8];
    for (int k = 0; k < 4; ++k) { wf[2 * k] = __uint_as_float(wv[k] << 16); wf[2 * k + 1] = __uint_as_float(wv[k] & 0xffff0000u); }
    for (int b = 0; b < 8; ++b) { float av = act[b * F + r]; for (int k = 0; k < 8; ++k) acc[b][k] += av * wf[k]; }
  }
  for (int b = 0; b < 8; ++b) for (int k = 0; k < 8; ++k) red[tid][b * 8 + k] = acc[b][k];
  __syncthreads();
  if (tid < 64) {
    float s = 0.f;
    for (int j = 0; j < 256; ++j) s += red[j][tid];
    int b = tid >> 3, k = tid & 7;
    out[b * D + c0 + k] = f2bf(hbuf[b * D + c0 + k] + rbf(s));
  }
}

extern "C" void kernel_launch(void* const* d_in, const int* in_sizes, int n_in, void* d_out, int out_size, void* d_ws,
                              size_t ws_size, hipStream_t stream) {
  const unsigned short* x = (const unsigned short*)d_in[0];
  const unsigned short* kc = (const unsigned short*)d_in[1];
  const unsigned short* vc = (const unsigned short*)d_in[2];
  const unsigned short* wq = (const unsigned short*)d_in[3];
  const unsigned short* wo = (const unsigned short*)d_in[4];
  const unsigned short* wg = (const unsigned short*)d_in[6];
  const unsigned short* wu = (const unsigned short*)d_in[7];
  const unsigned short* wd = (const unsigned short*)d_in[8];
  float* ws = (float*)d_ws;
  float* o = ws;
  float* hbuf = ws + 8 * 4096;
  float* act = hbuf + 8 * 640;
  k_attn<<<256, 512, 0, stream>>>(x, kc, vc, wq, o);
  k_wo<<<80, 256, 0, stream>>>(x, o, wo, hbuf);
  k_gu<<<216, 256, 0, stream>>>(hbuf, wg, wu, act);
  k_down<<<80, 256, 0, stream>>>(hbuf, act, wd, (unsigned short*)d_out);
}
```

```cpp
#include <hip/hip_runtime.h>
#include <stdint.h>

#define KW 5
#define KP 24
#define KC 6
#define KX 3
#define PACE 12
#define DIAG 0

typedef unsigned u32x4 __attribute__((ext_vector_type(4)));
typedef unsigned u32x2 __attribute__((ext_vector_type(2)));
typedef float f32x4 __attribute__((ext_vector_type(4)));
typedef float f32x2 __attribute__((ext_vector_type(2)));
typedef __bf16 bf16x2 __attribute__((ext_vector_type(2)));
typedef __bf16 bf16x8 __attribute__((ext_vector_type(8)));
typedef __amdgpu_buffer_rsrc_t rsrc_t;

constexpr int D = 640, SK = 4096, DH = 128, F = 1728, HD = 4096;
constexpr int NFFN = 27;
constexpr unsigned WS_G1 = 4096;
constexpr unsigned WS_G3 = WS_G1 + (2u << 20);
constexpr unsigned WS_G2 = WS_G3 + (2u << 20);
constexpr unsigned WS_DIAG = 8u << 20;
constexpr int AUX_NT = 2, AUX_SC1 = 16;
constexpr unsigned OOB = 0x80000000u;
constexpr int SPIN_CAP = 200000;

struct Lds {
  u32x4 down[4][20][64];
  float sc[4][16][64];
  float part[4][640];
  float qpart[4][128];
  float opart[4][128];
  float gpart[4][128];
  unsigned hn[704];
  float ml[4][2];
  unsigned tag;
};

__device__ __forceinline__ float bf_lo(unsigned u) { return __uint_as_float(u << 16); }
__device__ __forceinline__ float bf_hi(unsigned u) { return __uint_as_float(u & 0xffff0000u); }
__device__ __forceinline__ unsigned pk_bf(float a, float b) {
  bf16x2 v = {(__bf16)a, (__bf16)b};
  return __builtin_bit_cast(unsigned, v);
}
__device__ __forceinline__ float rbf(float f) { return bf_lo(pk_bf(f, f)); }
template <int CTRL> __device__ __forceinline__ float dppf(float v) {
  return __int_as_float(__builtin_amdgcn_update_dpp(0, __float_as_int(v), CTRL, 0xf, 0xf, true));
}
__device__ __forceinline__ float xrow_sum(float v) {
  auto r = __builtin_amdgcn_permlane16_swap(__float_as_uint(v), __float_as_uint(v), false, false);
  v = __uint_as_float(r[0]) + __uint_as_float(r[1]);
  auto s = __builtin_amdgcn_permlane32_swap(__float_as_uint(v), __float_as_uint(v), false, false);
  return __uint_as_float(s[0]) + __uint_as_float(s[1]);
}
__device__ __forceinline__ float wave_sum(float v) {
  v += dppf<0xB1>(v); v += dppf<0x4E>(v); v += dppf<0x141>(v); v += dppf<0x140>(v);
  return xrow_sum(v);
}
__device__ __forceinline__ float wave_max(float v) {
  v = fmaxf(v, dppf<0xB1>(v)); v = fmaxf(v, dppf<0x4E>(v)); v = fmaxf(v, dppf<0x141>(v)); v = fmaxf(v, dppf<0x140>(v));
  auto r = __builtin_amdgcn_permlane16_swap(__float_as_uint(v), __float_as_uint(v), false, false);
  v = fmaxf(__uint_as_float(r[0]), __uint_as_float(r[1]));
  auto s = __builtin_amdgcn_permlane32_swap(__float_as_uint(v), __float_as_uint(v), false, false);
  return fmaxf(__uint_as_float(s[0]), __uint_as_float(s[1]));
}
__device__ __forceinline__ float fexp(float x) { return __builtin_amdgcn_exp2f(x * 1.4426950408889634f); }
__device__ __forceinline__ void touch(const u32x4& v) { asm volatile("" ::"v"(v)); }
__device__ __forceinline__ void bar() {
  asm volatile("s_waitcnt lgkmcnt(0)\n\ts_barrier" ::: "memory");
}
__device__ __forceinline__ rsrc_t mk(const void* p, unsigned n) {
  return __builtin_amdgcn_make_buffer_rsrc((void*)p, 0, n, 0x00020000);
}
struct Masks { unsigned m[4]; };
template <int I> __device__ __forceinline__ u32x4 bsel(unsigned vec, const Masks& k) {
  unsigned b = (unsigned)__builtin_amdgcn_mov_dpp((int)vec, 0x150 + I, 0xf, 0xf, true);
  u32x4 r = {b & k.m[0], b & k.m[1], b & k.m[2], b & k.m[3]};
  return r;
}
__device__ __forceinline__ f32x4 mm(const u32x4& a, const u32x4& b, f32x4 c) {
  return __builtin_amdgcn_mfma_f32_16x16x32_bf16(__builtin_bit_cast(bf16x8, a), __builtin_bit_cast(bf16x8, b), c, 0, 0, 0);
}
constexpr int rev4(int i) { return ((i & 1) << 3) | ((i & 2) << 1) | ((i & 4) >> 1) | ((i & 8) >> 3); }
constexpr int sched(int j, int pre) { int a = j + KW + 1, b = pre + (j + KC) / KC; return a > b ? a : b; }
__device__ __forceinline__ int pos2col(int q) { return (q & ~127) + ((q >> 5) & 3) * 32 + (q & 3) * 8 + ((q >> 2) & 7); }

template <int I, int N, class Fn> __device__ __forceinline__ void sfor(Fn&& f) {
  if constexpr (I < N) { f(std::integral_constant<int, I>{}); sfor<I + 1, N>(f); }
}

template <int I> __device__ __forceinline__ void kstep(const u32x4& kv, const unsigned (&q)[4], float (&t)[5], bool b3, bool b2,
                                                       bool b1, bool b0) {
  float d = 0.f;
#pragma unroll
  for (int k = 0; k < 4; ++k) {
    const unsigned e = kv[k], f = q[k];
    d = __builtin_amdgcn_fdot2_f32_bf16(__builtin_bit_cast(bf16x2, e), __builtin_bit_cast(bf16x2, f), d, false);
  }
  d += dppf<0x128>(d);
  if constexpr ((I & 1) == 0) { t[0] = d; return; }
  d = b3 ? d : t[0];
  d += dppf<0x141>(d);
  if constexpr ((I & 2) == 0) { t[1] = d; return; }
  d = b2 ? d : t[1];
  d += dppf<0x4E>(d);
  if constexpr ((I & 4) == 0) { t[2] = d; return; }
  d = b1 ? d : t[2];
  d += dppf<0xB1>(d);
  if constexpr ((I & 8) == 0) { t[3] = d; return; }
  t[4] = b0 ? d : t[3];
}

#define STAMP(i)

__global__ void __launch_bounds__(512, 2)
fused_block(const unsigned short* __restrict__ x, const unsigned short* __restrict__ kc, const unsigned short* __restrict__ vc,
            const unsigned short* __restrict__ wq, const unsigned short* __restrict__ wo, const unsigned short* __restrict__ wg,
            const unsigned short* __restrict__ wu, const unsigned short* __restrict__ wd, unsigned short* __restrict__ out,
            unsigned char* __restrict__ ws) {
  __shared__ Lds L;
  const int tid = threadIdx.x, lane = tid & 63, g = lane >> 4, c = lane & 15;
  const int wave = __builtin_amdgcn_readfirstlane(tid >> 6);
  const int bh = blockIdx.x, b = bh >> 5, h = bh & 31;
  const bool ffn = h < NFFN;
  STAMP(0);
  Masks mk4;
#pragma unroll
  for (int j = 0; j < 4; ++j) mk4.m[j] = ((c >> 1) == j) ? ((c & 1) ? 0xffff0000u : 0x0000ffffu) : 0u;

  if (wave < 4) {
    const int w = wave;
    const unsigned char* kb = (const unsigned char*)kc + (size_t)bh * (SK * DH * 2);
    const unsigned char* vb = (const unsigned char*)vc + (size_t)bh * (SK * DH * 2);
    const rsrc_t rK = mk(kb, SK * DH * 2), rV = mk(vb, SK * DH * 2);
    const unsigned voff = lane * 16 + w * 1024;
    u32x4 ring[8], pre[32];
    auto kslot = [&](auto J) -> u32x4& { if constexpr (J.value < 32) return pre[J.value]; else return ring[J.value & 7]; };
    auto kissue = [&](auto J) {
      if constexpr (J.value >= KW) touch(kslot(std::integral_constant<int, J.value - KW>{}));
      kslot(J) = __builtin_amdgcn_raw_buffer_load_b128(rK, voff, J.value * 4096, AUX_NT);
      __builtin_amdgcn_sched_barrier(0);
    };
    sfor<0, KP>(kissue);
    STAMP(1);
    bar();
    STAMP(2);
    const unsigned tag = L.tag;
    unsigned q[4];
    {
      f32x4 a0 = {0, 0, 0, 0}, a1 = {0, 0, 0, 0};
#pragma unroll
      for (int s = 0; s < 4; ++s) {
        a0 += *(const f32x4*)&L.qpart[s][8 * c];
        a1 += *(const f32x4*)&L.qpart[s][8 * c + 4];
      }
      q[0] = pk_bf(a0[0], a0[1]); q[1] = pk_bf(a0[2], a0[3]); q[2] = pk_bf(a1[0], a1[1]); q[3] = pk_bf(a1[2], a1[3]);
    }
    const bool b3 = c & 8, b2 = c & 4, b1 = c & 2, b0 = c & 1;
    float t[5], m, l;
    auto score = [&](int G, bool first) {
      const float s = rbf(rbf(t[4]) * 0.08838834764831845f);
      L.sc[w][G][lane] = s;
      if (first) { m = s; l = 1.f; }
      else { const float mn = fmaxf(m, s); l = l * fexp(m - mn) + fexp(s - mn); m = mn; }
    };
    sfor<0, 32>([&](auto J) {
      constexpr int lo = J.value == 0 ? KP : sched(J.value - 1, KP), hi = sched(J.value, KP);
      sfor<lo, hi>(kissue);
      kstep<J.value & 15>(pre[J.value], q, t, b3, b2, b1, b0);
      if constexpr ((J.value & 15) == 15) score(J.value >> 4, J.value == 15);
      __builtin_amdgcn_sched_barrier(0);
    });
    static_assert(sched(31, KP) == 32 + KW, "backlog must be drained inside the unrolled groups");
    for (int G = 2; G < 16; ++G) {
      const bool last = G == 15;
      const rsrc_t rN = mk(last ? vb : kb, SK * DH * 2);
      const int so = G * 65536, sn = last ? 0 : so + 65536;
      sfor<0, 16>([&](auto I) {
        constexpr int i = I.value;
        touch(ring[i & 7]);
        if constexpr (i + KW < 16) ring[(i + KW) & 7] = __builtin_amdgcn_raw_buffer_load_b128(rK, voff, so + (i + KW) * 4096, AUX_NT);
        else ring[(i + KW) & 7] = __builtin_amdgcn_raw_buffer_load_b128(rN, voff, sn + (i + KW - 16) * 4096, AUX_NT);
        __builtin_amdgcn_sched_barrier(0);
        kstep<i>(ring[i & 7], q, t, b3, b2, b1, b0);
        if constexpr (i == 15) score(G, false);
        __builtin_amdgcn_sched_barrier(0);
      });
    }
    STAMP(3);
    u32x4 vpre[16];
    auto vslot = [&](auto J) -> u32x4& { if constexpr (J.value >= KW && J.value < 16) return vpre[J.value]; else return ring[J.value & 7]; };
    auto vissue = [&](auto J) {
      touch(vslot(std::integral_constant<int, J.value - KW>{}));
      vslot(J) = __builtin_amdgcn_raw_buffer_load_b128(rV, voff, J.value * 4096, AUX_NT);
      __builtin_amdgcn_sched_barrier(0);
    };
    float snext = L.sc[w][0][lane];
    if constexpr (KX >= 1) vissue(std::integral_constant<int, KW>{});
    const float mw = wave_max(m);
    const float lw = wave_sum(l * fexp(m - mw));
    if (lane == 0) { L.ml[w][0] = mw; L.ml[w][1] = lw; }
    __builtin_amdgcn_sched_barrier(0);
    sfor<KW + 1, KW + KX>(vissue);
    bar();
    STAMP(4);
    float M, inv;
    {
      const f32x4 a = *(const f32x4*)&L.ml[0][0], e = *(const f32x4*)&L.ml[2][0];
      M = fmaxf(fmaxf(a[0], a[2]), fmaxf(e[0], e[2]));
      const float tot = a[1] * fexp(a[0] - M) + a[3] * fexp(a[2] - M) + e[1] * fexp(e[0] - M) + e[3] * fexp(e[2] - M);
      inv = 1.f / tot;
    }
    auto prob = [&](float s) { const float p = fexp(s - M) * inv; return pk_bf(p, p); };
    unsigned pp = prob(snext), pn;
    f32x4 acc0 = {0, 0, 0, 0}, acc1 = {0, 0, 0, 0};
    auto vstep = [&](auto I, const u32x4& v) {
      constexpr int i = I.value;
      const u32x4 B = bsel<rev4(i)>(pp, mk4);
      if constexpr (i & 1) acc1 = mm(v, B, acc1); else acc0 = mm(v, B, acc0);
    };
    sfor<0, 16>([&](auto J) {
      constexpr int lo = J.value == 0 ? KW + KX : sched(J.value - 1, KW + KX), hi = sched(J.value, KW + KX);
      sfor<lo, hi>(vissue);
      if constexpr (J.value == 0) snext = L.sc[w][1][lane];
      if constexpr (J.value == 8) pn = prob(snext);
      vstep(J, vslot(J));
      __builtin_amdgcn_sched_barrier(0);
    });
    static_assert(sched(15, KW + KX) == 16 + KW, "V backlog must be drained inside group 0");
    pp = pn;
    u32x4 gu[40];
    auto vgroup = [&](int G, auto EX) {
      constexpr int ex = EX.value;
      const bool last = G == 15;
      const rsrc_t rN = mk(vb, last ? 0 : SK * DH * 2);
      const int so = G * 65536, sn = last ? 0 : so + 65536;
      const unsigned short* wp = nullptr;
      if constexpr (ex >= 0) wp = (c < 8 ? wg : wu) + (size_t)(160 * w + 8 * g) * F + 64 * h + 8 * (c & 7);
      sfor<0, 16>([&](auto I) {
        constexpr int i = I.value;
        touch(ring[i & 7]);
        if constexpr (i + KW < 16) ring[(i + KW) & 7] = __builtin_amdgcn_raw_buffer_load_b128(rV, voff, so + (i + KW) * 4096, AUX_NT);
        else ring[(i + KW) & 7] = __builtin_amdgcn_raw_buffer_load_b128(rN, voff, sn + (i + KW - 16) * 4096, AUX_NT);
        if constexpr (ex >= 0) {
          sfor<ex + (i * 20) / 16, ex + ((i + 1) * 20) / 16>([&](auto K) {
            constexpr int k = K.value;
            gu[k] = *(const u32x4*)(wp + (32 * (k >> 3) + (k & 7)) * F);
          });
        }
        __builtin_amdgcn_sched_barrier(0);
        if constexpr (i == 0) snext = L.sc[w][last ? 15 : G + 1][lane];
        if constexpr (i == 8) pn = prob(snext);
        vstep(I, ring[i & 7]);
        __builtin_amdgcn_sched_barrier(0);
      });
      pp = pn;
    };
    int G0 = 1;
    if (ffn) {
      vgroup(1, std::integral_constant<int, 0>{});
      vgroup(2, std::integral_constant<int, 20>{});
      G0 = 3;
    }
    for (int G = G0; G < 16; ++G) vgroup(G, std::integral_constant<int, -1>{});
    acc0 += acc1;
    if (c < 8) *(f32x4*)&L.opart[w][(g * 8 + c) * 4] = acc0;
    STAMP(5);
    bar();
    bar();
    STAMP(6);
    const unsigned pj = tid / 10, ps = tid - pj * 10;
    {
      f32x2 s = *(const f32x2*)&L.part[0][2 * tid];
#pragma unroll
      for (int k = 1; k < 4; ++k) s += *(const f32x2*)&L.part[k][2 * tid];
      const u32x4 gr = {__float_as_uint(s[0]), tag, __float_as_uint(s[1]), tag};
      __builtin_amdgcn_raw_buffer_store_b128(gr, mk(ws + WS_G1 + b * 262144, 262144), pj * 8192 + ps * 16, h * 256, AUX_SC1);
    }
    if (ffn) {
      bar();
      STAMP(7);
      unsigned hv[3];
#pragma unroll
      for (int r = 0; r < 3; ++r) hv[r] = L.hn[160 * w + 64 * r + 4 * c + g];
      f32x4 a0 = {0, 0, 0, 0}, a1 = {0, 0, 0, 0};
      sfor<0, 40>([&](auto K) {
        constexpr int k = K.value;
        const u32x4 B = bsel<k & 15>(hv[k >> 4], mk4);
        if constexpr (k & 1) a1 = mm(gu[k], B, a1); else a0 = mm(gu[k], B, a0);
      });
      a0 += a1;
      if (c < 8) *(f32x4*)&L.gpart[w][(g * 8 + c) * 4] = a0;
      STAMP(8);
      bar();
      bar();
      STAMP(9);
      f32x2 s = *(const f32x2*)&L.part[0][2 * tid];
#pragma unroll
      for (int k = 1; k < 4; ++k) s += *(const f32x2*)&L.part[k][2 * tid];
      const u32x4 gr = {__float_as_uint(s[0]), tag, __float_as_uint(s[1]), tag};
      __builtin_amdgcn_raw_buffer_store_b128(gr, mk(ws + WS_G3 + b * 262144, 262144), pj * 8192 + ps * 16, h * 256, AUX_SC1);
    }
  } else {
    const int w = wave - 4;
    const rsrc_t rE = mk(ws + b * 256, 256);
    const unsigned tag = __builtin_amdgcn_raw_buffer_load_b32(rE, 0, 0, AUX_SC1) + 1u;
    {
      const unsigned* x32 = (const unsigned*)(x + b * D);
      unsigned xs[5];
#pragma unroll
      for (int k = 0; k < 5; ++k) xs[k] = x32[lane + 64 * k];
      unsigned xe[3];
#pragma unroll
      for (int r = 0; r < 3; ++r) xe[r] = x[b * D + 160 * w + (r < 2 ? 64 * r + 4 * c : 128 + 4 * (c & 7)) + g];
      const rsrc_t rQ = mk(wq + (size_t)160 * w * HD + h * DH, 160 * HD * 2);
      u32x4 wf[40];
      sfor<0, 40>([&](auto K) { wf[K.value] = __builtin_amdgcn_raw_buffer_load_b128(rQ, g * (HD * 2) + c * 16, K.value * 4 * HD * 2, 0); });
      __builtin_amdgcn_sched_barrier(0);
      if (tid == 256) L.tag = tag;
      float s = 0.f;
#pragma unroll
      for (int k = 0; k < 5; ++k) s += bf_lo(xs[k]) + bf_hi(xs[k]);
      const float mean = wave_sum(s) * (1.f / D);
      float v = 0.f;
#pragma unroll
      for (int k = 0; k < 5; ++k) { const float a = bf_lo(xs[k]) - mean, e = bf_hi(xs[k]) - mean; v += a * a + e * e; }
      const float var = wave_sum(v) * (1.f / D);
      const float mb = rbf(mean), den = rbf(sqrtf(rbf(rbf(var) + 1e-5f)));
      unsigned xv[3];
#pragma unroll
      for (int r = 0; r < 3; ++r) { const float n = rbf(bf_lo(xe[r]) - mb) / den; xv[r] = pk_bf(n, n); }
      f32x4 a0 = {0, 0, 0, 0}, a1 = {0, 0, 0, 0};
      sfor<0, 40>([&](auto K) {
        constexpr int k = K.value;
        const u32x4 B = bsel<k & 15>(xv[k >> 4], mk4);
        if constexpr (k & 1) a1 = mm(wf[k], B, a1); else a0 = mm(wf[k], B, a0);
      });
      a0 += a1;
      if (c < 8) {
#pragma unroll
        for (int j = 0; j < 4; ++j) L.qpart[w][32 * g + 8 * j + c] = a0[j];
      }
    }
    STAMP(1);
    bar();
    const unsigned wvoff = g * (8 * D * 2) + c * 16;
    u32x4 wf[40];
    {
      const rsrc_t rO = mk(wo + (size_t)(128 * h + 32 * w) * D, 32 * D * 2);
      const rsrc_t rD = mk(wd + (size_t)(64 * (ffn ? h : 0) + 32 * (w >> 1) + 4 * (w & 1)) * D, ffn ? 28 * D * 2 : 0);
      sfor<0, 40>([&](auto K) {
        constexpr int k = K.value;
        wf[k] = __builtin_amdgcn_raw_buffer_load_b128(rO, wvoff, (k / 5) * D * 2 + (k % 5) * 256, 0);
        if constexpr (k < 20) L.down[w][k][lane] = __builtin_amdgcn_raw_buffer_load_b128(rD, wvoff, (k / 5) * D * 2 + (k % 5) * 256, 0);
        __builtin_amdgcn_s_sleep(PACE);
        __builtin_amdgcn_sched_barrier(0);
      });
    }
    const int q0 = 20 * h + 2 * (c < 10 ? c : 0);
    const int col0 = pos2col(q0);
    const float x0 = bf_lo(x[b * D + col0]), x1 = bf_lo(x[b * D + col0 + 8]);
    STAMP(2);
    bar();
    bar();
    STAMP(5);
    {
      float o = L.opart[0][32 * w + 4 * (c & 7) + g];
#pragma unroll
      for (int s = 1; s < 4; ++s) o += L.opart[s][32 * w + 4 * (c & 7) + g];
      const unsigned ov = pk_bf(o, o);
      f32x4 a[5];
#pragma unroll
      for (int t = 0; t < 5; ++t) a[t] = f32x4{0, 0, 0, 0};
      sfor<0, 8>([&](auto I) {
        const u32x4 B = bsel<I.value>(ov, mk4);
#pragma unroll
        for (int t = 0; t < 5; ++t) a[t] = mm(wf[I.value * 5 + t], B, a[t]);
      });
      if (c < 8) {
#pragma unroll
        for (int t = 0; t < 5; ++t) *(f32x4*)&L.part[w][128 * t + (g * 8 + c) * 4] = a[t];
      }
    }
    bar();
    STAMP(6);
    const unsigned pj = tid / 10, ps = tid - pj * 10;
    if (w == 0) {
      f32x2 s = *(const f32x2*)&L.part[0][2 * tid];
#pragma unroll
      for (int k = 1; k < 4; ++k) s += *(const f32x2*)&L.part[k][2 * tid];
      const u32x4 gr = {__float_as_uint(s[0]), tag, __float_as_uint(s[1]), tag};
      __builtin_amdgcn_raw_buffer_store_b128(gr, mk(ws + WS_G1 + b * 262144, 262144), pj * 8192 + ps * 16, h * 256, AUX_SC1);
    }
    sfor<0, 20>([&](auto K) { wf[K.value] = L.down[w][K.value][lane]; });
    float h0 = 0.f, h1 = 0.f;
    const unsigned gvoff = c < 10 ? g * 256 + c * 16 : OOB;
    if (w == 1) {
      const rsrc_t r = mk(ws + WS_G1 + bh * 8192, 8192);
      u32x4 v[8];
      for (int spin = 0; spin < SPIN_CAP; ++spin) {
        asm volatile("" ::: "memory");
#pragma unroll
        for (int k = 0; k < 8; ++k) v[k] = __builtin_amdgcn_raw_buffer_load_b128(r, gvoff, k * 1024, AUX_SC1);
        bool ok = true;
#pragma unroll
        for (int k = 0; k < 8; ++k) ok &= v[k][1] == tag && v[k][3] == tag;
        if (__all(ok || c >= 10)) break;
        __builtin_amdgcn_s_sleep(1);
      }
      float s0 = 0.f, s1 = 0.f;
#pragma unroll
      for (int k = 0; k < 8; ++k) { s0 += __uint_as_float(v[k][0]); s1 += __uint_as_float(v[k][2]); }
      h0 = rbf(x0 + rbf(xrow_sum(s0)));
      h1 = rbf(x1 + rbf(xrow_sum(s1)));
      const u32x2 gr = {pk_bf(h0, h1), tag};
      __builtin_amdgcn_raw_buffer_store_b64(gr, mk(ws + WS_G2 + bh * 128, 128), lane < 10 ? lane * 8 : OOB, 0, AUX_SC1);
      if (h == 0) __builtin_amdgcn_raw_buffer_store_b32(tag, rE, lane == 0 ? 0 : OOB, 0, AUX_SC1);
      STAMP(7);
    }
    if (ffn) {
      if (w == 2) {
        const rsrc_t r = mk(ws + WS_G2 + b * 4096, 4096);
        unsigned po[3];
#pragma unroll
        for (int k = 0; k < 3; ++k) { const unsigned p = 64 * k + lane, j = p / 5; po[k] = p < 160 ? j * 128 + (p - j * 5) * 16 : OOB; }
        u32x4 v[3];
        for (int spin = 0; spin < SPIN_CAP; ++spin) {
          asm volatile("" ::: "memory");
#pragma unroll
          for (int k = 0; k < 3; ++k) v[k] = __builtin_amdgcn_raw_buffer_load_b128(r, po[k], 0, AUX_SC1);
          const bool ok = v[0][1] == tag && v[0][3] == tag && v[1][1] == tag && v[1][3] == tag &&
                          ((v[2][1] == tag && v[2][3] == tag) || lane >= 32);
          if (__all(ok)) break;
          __builtin_amdgcn_s_sleep(1);
        }
        STAMP(7);
        float e[12];
#pragma unroll
        for (int k = 0; k < 3; ++k) {
          e[4 * k] = bf_lo(v[k][0]); e[4 * k + 1] = bf_hi(v[k][0]); e[4 * k + 2] = bf_lo(v[k][2]); e[4 * k + 3] = bf_hi(v[k][2]);
        }
        float s = 0.f;
#pragma unroll
        for (int k = 0; k < 12; ++k) s += e[k];
        const float mean = wave_sum(s) * (1.f / D);
        float va = 0.f, vb2 = 0.f;
#pragma unroll
        for (int k = 0; k < 8; ++k) { const float a = e[k] - mean; va += a * a; }
#pragma unroll
        for (int k = 8; k < 12; ++k) { const float a = e[k] - mean; vb2 += a * a; }
        const float var = wave_sum(lane < 32 ? va + vb2 : va) * (1.f / D);
        const float mb = rbf(mean), rden = __builtin_amdgcn_rcpf(rbf(__builtin_amdgcn_sqrtf(rbf(rbf(var) + 1e-5f))));
#pragma unroll
        for (int k = 0; k < 3; ++k) {
          u32x4 o;
#pragma unroll
          for (int j = 0; j < 4; ++j) { const float n = rbf(e[4 * k + j] - mb) * rden; o[j] = pk_bf(n, n); }
          if (k < 2 || lane < 32) *(u32x4*)&L.hn[4 * (64 * k + lane)] = o;
        }
        STAMP(8);
      }
      bar();
      bar();
      if (w != 2) STAMP(8);
      const int u = 16 * w + 4 * (c & 3) + g;
      float sg = L.gpart[0][u], su = L.gpart[0][64 + u];
#pragma unroll
      for (int s = 1; s < 4; ++s) { sg += L.gpart[s][u]; su += L.gpart[s][64 + u]; }
      sg = rbf(sg);
      const float act = rbf(sg * __builtin_amdgcn_rcpf(1.f + fexp(-sg))) * rbf(su);
      const unsigned av = pk_bf(act, act);
      f32x4 a[5];
#pragma unroll
      for (int t = 0; t < 5; ++t) a[t] = f32x4{0, 0, 0, 0};
      sfor<0, 4>([&](auto I) {
        const u32x4 B = bsel<I.value>(av, mk4);
#pragma unroll
        for (int t = 0; t < 5; ++t) a[t] = mm(wf[I.value * 5 + t], B, a[t]);
      });
      if (c < 8) {
#pragma unroll
        for (int t = 0; t < 5; ++t) *(f32x4*)&L.part[w][128 * t + (g * 8 + c) * 4] = a[t];
      }
      bar();
      STAMP(9);
      if (w == 0) {
        f32x2 s = *(const f32x2*)&L.part[0][2 * tid];
#pragma unroll
        for (int k = 1; k < 4; ++k) s += *(const f32x2*)&L.part[k][2 * tid];
        const u32x4 gr = {__float_as_uint(s[0]), tag, __float_as_uint(s[1]), tag};
        __builtin_amdgcn_raw_buffer_store_b128(gr, mk(ws + WS_G3 + b * 262144, 262144), pj * 8192 + ps * 16, h * 256, AUX_SC1);
      }
    }
    if (w == 1) {
      const rsrc_t r = mk(ws + WS_G3 + bh * 8192, 8192);
      const unsigned lvoff = g == 3 ? OOB : gvoff;
      u32x4 v[7];
      for (int spin = 0; spin < SPIN_CAP; ++spin) {
        asm volatile("" ::: "memory");
#pragma unroll
        for (int k = 0; k < 7; ++k) v[k] = __builtin_amdgcn_raw_buffer_load_b128(r, k < 6 ? gvoff : lvoff, k * 1024, AUX_SC1);
        bool ok = (v[6][1] == tag && v[6][3] == tag) || g == 3;
#pragma unroll
        for (int k = 0; k < 6; ++k) ok &= v[k][1] == tag && v[k][3] == tag;
        if (__all(ok || c >= 10)) break;
        __builtin_amdgcn_s_sleep(1);
      }
      STAMP(10);
      float s0 = 0.f, s1 = 0.f;
#pragma unroll
      for (int k = 0; k < 7; ++k) { s0 += __uint_as_float(v[k][0]); s1 += __uint_as_float(v[k][2]); }
      const unsigned o = pk_bf(h0 + rbf(xrow_sum(s0)), h1 + rbf(xrow_sum(s1)));
      if (lane < 10) {
        out[b * D + col0] = (unsigned short)o;
        out[b * D + col0 + 8] = (unsigned short)(o >> 16);
      }
    }
  }
}

extern "C" void kernel_launch(void* const* d_in, const int* in_sizes, int n_in, void* d_out, int out_size, void* d_ws,
                              size_t ws_size, hipStream_t stream) {
  fused_block<<<256, 512, 0, stream>>>((const unsigned short*)d_in[0], (const unsigned short*)d_in[1], (const unsigned short*)d_in[2],
                                       (const unsigned short*)d_in[3], (const unsigned short*)d_in[4], (const unsigned short*)d_in[6],
                                       (const unsigned short*)d_in[7], (const unsigned short*)d_in[8], (unsigned short*)d_out,
                                       (unsigned char*)d_ws);
}
```

```cpp
#include <hip/hip_runtime.h>
#include <stdint.h>

constexpr int TS = 7050;
constexpr int T0 = 60;
constexpr int PSTEP = 2;
constexpr int SLEW = 2;
constexpr int EKN = 6;
constexpr int KW = 6;
constexpr int KD = 8;
constexpr int LAGT = 25;
constexpr int KP = 40;
constexpr int KWI = 8;
constexpr int KC = 4;
constexpr int KX = 3;
constexpr int QT = 400;
constexpr int W0 = 800;
constexpr int LP = 500;
constexpr int CE = 2;
constexpr int LR = 150;
constexpr int DLT = 10;
constexpr int DLT0 = 30;
constexpr int LO = 160;
constexpr int LS = 3;
constexpr int LW = 12;
constexpr int LJ = 48;
constexpr int SD = 250;
constexpr int GE = 13;
constexpr int GB = 6;
constexpr int GP = 5;
constexpr int GT = 34;
constexpr int DL1 = 40, DL2 = 25, DL12 = 145, DL3 = 50, DL23 = 170;
constexpr unsigned long long MAPX = 0x58360127ba49ull;

typedef unsigned u32x4 __attribute__((ext_vector_type(4)));
typedef unsigned u32x2 __attribute__((ext_vector_type(2)));
typedef float f32x4 __attribute__((ext_vector_type(4)));
typedef float f32x2 __attribute__((ext_vector_type(2)));
typedef __bf16 bf16x2 __attribute__((ext_vector_type(2)));
typedef __bf16 bf16x8 __attribute__((ext_vector_type(8)));
typedef __amdgpu_buffer_rsrc_t rsrc_t;

constexpr int D = 640, SK = 4096, DH = 128, F = 1728, HD = 4096;
constexpr int NFFN = 27;
constexpr unsigned WS_ST = 2048;
constexpr unsigned WS_G1 = 4096;
constexpr unsigned WS_G3 = WS_G1 + (1u << 20);
constexpr unsigned WS_G2 = WS_G3 + (1u << 20);
constexpr unsigned WS_QT = WS_G2 + (64u << 10);
constexpr unsigned WS_Q = WS_QT + (64u << 10);
constexpr unsigned WS_PR = WS_Q + (1u << 20);
constexpr int AUX_NT = 2, AUX_SC1 = 16;
constexpr unsigned OOB = 0x80000000u;
constexpr int SPIN_CAP = 200000;

struct Lds {
  u32x4 down[4][20][64];
  float sc[4][16][64];
  float part[4][640];
  float qpart[4][128];
  float opart[4][128];
  float gpart[4][128];
  unsigned hn[704];
  float ml[4][2];
  unsigned tag, cnt, base, cntm;
  unsigned at[4];
};

__device__ __forceinline__ float bf_lo(unsigned u) { return __uint_as_float(u << 16); }
__device__ __forceinline__ float bf_hi(unsigned u) { return __uint_as_float(u & 0xffff0000u); }
__device__ __forceinline__ unsigned pk_bf(float a, float b) {
  bf16x2 v = {(__bf16)a, (__bf16)b};
  return __builtin_bit_cast(unsigned, v);
}
__device__ __forceinline__ float rbf(float f) { return bf_lo(pk_bf(f, f)); }
template <int DC> __device__ __forceinline__ float dppf(float v) {
  return __int_as_float(__builtin_amdgcn_update_dpp(0, __float_as_int(v), DC, 0xf, 0xf, true));
}
__device__ __forceinline__ float xrow_sum(float v) {
  auto r = __builtin_amdgcn_permlane16_swap(__float_as_uint(v), __float_as_uint(v), false, false);
  v = __uint_as_float(r[0]) + __uint_as_float(r[1]);
  auto s = __builtin_amdgcn_permlane32_swap(__float_as_uint(v), __float_as_uint(v), false, false);
  return __uint_as_float(s[0]) + __uint_as_float(s[1]);
}
__device__ __forceinline__ float wave_sum(float v) {
  v += dppf<0xB1>(v); v += dppf<0x4E>(v); v += dppf<0x141>(v); v += dppf<0x140>(v);
  return xrow_sum(v);
}
__device__ __forceinline__ float half_sum(float v) {
  v += dppf<0xB1>(v); v += dppf<0x4E>(v); v += dppf<0x141>(v); v += dppf<0x140>(v);
  auto r = __builtin_amdgcn_permlane16_swap(__float_as_uint(v), __float_as_uint(v), false, false);
  return __uint_as_float(r[0]) + __uint_as_float(r[1]);
}
__device__ __forceinline__ float rdl(float v, int l) { return __int_as_float(__builtin_amdgcn_readlane(__float_as_int(v), l)); }
__device__ __forceinline__ float wave_max(float v) {
  v = fmaxf(v, dppf<0xB1>(v)); v = fmaxf(v, dppf<0x4E>(v)); v = fmaxf(v, dppf<0x141>(v)); v = fmaxf(v, dppf<0x140>(v));
  auto r = __builtin_amdgcn_permlane16_swap(__float_as_uint(v), __float_as_uint(v), false, false);
  v = fmaxf(__uint_as_float(r[0]), __uint_as_float(r[1]));
  auto s = __builtin_amdgcn_permlane32_swap(__float_as_uint(v), __float_as_uint(v), false, false);
  return fmaxf(__uint_as_float(s[0]), __uint_as_float(s[1]));
}
__device__ __forceinline__ float fexp(float x) { return __builtin_amdgcn_exp2f(x * 1.4426950408889634f); }
__device__ __forceinline__ void touch(const u32x4& v) { asm volatile("" ::"v"(v)); }
__device__ __forceinline__ void bar() {
  asm volatile("s_waitcnt lgkmcnt(0)\n\ts_barrier" ::: "memory");
}
__device__ __forceinline__ rsrc_t mk(const void* p, unsigned n) {
  return __builtin_amdgcn_make_buffer_rsrc((void*)p, 0, n, 0x00020000);
}
typedef short s16x4 __attribute__((ext_vector_type(4)));
__device__ __forceinline__ u32x2 trd(const unsigned char* p) {
  return __builtin_bit_cast(u32x2, __builtin_amdgcn_ds_read_tr16_b64_v4i16((__attribute__((address_space(3))) s16x4*)p));
}
__device__ __forceinline__ unsigned now() { return (unsigned)__builtin_amdgcn_s_memrealtime(); }
__device__ __forceinline__ void wait_until(unsigned t) {
#pragma unroll 1
  for (int k = 0; k < 4096 && (int)(now() - t) < 0; ++k) {}
}
__device__ __forceinline__ u32x4 seal(const f32x4& v, unsigned tag) {
  u32x4 r;
#pragma unroll
  for (int e = 0; e < 4; ++e) r[e] = __builtin_amdgcn_perm(__float_as_uint(v[e]) + 0x80u, tag, (e & 1) ? 0x07060501u : 0x07060500u);
  return r;
}
__device__ __forceinline__ unsigned seal_of(const u32x4& v) {
  return __builtin_amdgcn_perm(v[1], v[0], 0x0c0c0400u) | __builtin_amdgcn_perm(v[3], v[2], 0x04000c0cu);
}
__device__ __forceinline__ float unseal_sum(const u32x4 (&v)[4]) {
  float s[4];
#pragma unroll
  for (int e = 0; e < 4; ++e)
    s[e] = (__uint_as_float(v[0][e] & 0xffffff00u) + __uint_as_float(v[1][e] & 0xffffff00u)) +
           (__uint_as_float(v[2][e] & 0xffffff00u) + __uint_as_float(v[3][e] & 0xffffff00u));
  auto p = __builtin_amdgcn_permlane16_swap(__float_as_uint(s[0]), __float_as_uint(s[1]), false, false);
  auto q = __builtin_amdgcn_permlane16_swap(__float_as_uint(s[2]), __float_as_uint(s[3]), false, false);
  const float u = __uint_as_float(p[0]) + __uint_as_float(p[1]), w = __uint_as_float(q[0]) + __uint_as_float(q[1]);
  auto r = __builtin_amdgcn_permlane32_swap(__float_as_uint(u), __float_as_uint(w), false, false);
  const float z = __uint_as_float(r[0]) + __uint_as_float(r[1]);
  return z + dppf<0x128>(z);
}
struct Masks { unsigned m[4]; };
template <int I> __device__ __forceinline__ u32x4 bsel(unsigned vec, const Masks& k) {
  unsigned b = (unsigned)__builtin_amdgcn_mov_dpp((int)vec, 0x150 + I, 0xf, 0xf, true);
  u32x4 r = {b & k.m[0], b & k.m[1], b & k.m[2], b & k.m[3]};
  return r;
}
__device__ __forceinline__ f32x4 mm(const u32x4& a, const u32x4& b, f32x4 c) {
  return __builtin_amdgcn_mfma_f32_16x16x32_bf16(__builtin_bit_cast(bf16x8, a), __builtin_bit_cast(bf16x8, b), c, 0, 0, 0);
}
constexpr int mbit(int k) { return 8 + (int)((MAPX >> (44 - 4 * k)) & 15); }
__device__ __forceinline__ constexpr int spread(int v, int k, int n) {
  int r = 0;
  for (int j = 0; j < n; ++j) r |= ((v >> j) & 1) << mbit(k + j);
  return r;
}
constexpr int offi(int i) { return spread(i, 4, 4); }
__device__ __forceinline__ constexpr int offg(int G) { return spread(G, 8, 4); }
constexpr int rev4(int i) { return ((i & 1) << 3) | ((i & 2) << 1) | ((i & 4) >> 1) | ((i & 8) >> 3); }
constexpr int sched(int j, int pre) { int a = j + KD + 1, b = pre + (j + KC) / KC; return a > b ? a : b; }
__device__ __forceinline__ int pos2col(int q) { return (q & ~127) + ((q >> 5) & 3) * 32 + (q & 3) * 8 + ((q >> 2) & 7); }

__device__ __forceinline__ constexpr unsigned due(int n) { return ((unsigned)n * TS) >> 9; }
template <int I, int N, class Fn> __device__ __forceinline__ void sfor(Fn&& f) {
  if constexpr (I < N) { f(std::integral_constant<int, I>{}); sfor<I + 1, N>(f); }
}

template <int I> __device__ __forceinline__ void kstep(const u32x4& kv, const unsigned (&q)[4], float (&t)[5], bool b3, bool b2,
                                                       bool b1, bool b0) {
  float d = 0.f;
#pragma unroll
  for (int k = 0; k < 4; ++k) {
    const unsigned e = kv[k], f = q[k];
    d = __builtin_amdgcn_fdot2_f32_bf16(__builtin_bit_cast(bf16x2, e), __builtin_bit_cast(bf16x2, f), d, false);
  }
  d += dppf<0x128>(d);
  if constexpr ((I & 1) == 0) { t[0] = d; return; }
  d = b3 ? d : t[0];
  d += dppf<0x141>(d);
  if constexpr ((I & 2) == 0) { t[1] = d; return; }
  d = b2 ? d : t[1];
  d += dppf<0x4E>(d);
  if constexpr ((I & 4) == 0) { t[2] = d; return; }
  d = b1 ? d : t[2];
  d += dppf<0xB1>(d);
  if constexpr ((I & 8) == 0) { t[3] = d; return; }
  t[4] = b0 ? d : t[3];
}


__global__ void __launch_bounds__(512, 2)
fused_block(const unsigned short* __restrict__ x, const unsigned short* __restrict__ kc, const unsigned short* __restrict__ vc,
            const unsigned short* __restrict__ wq, const unsigned short* __restrict__ wo, const unsigned short* __restrict__ wg,
            const unsigned short* __restrict__ wu, const unsigned short* __restrict__ wd, unsigned short* __restrict__ out,
            unsigned char* __restrict__ ws) {
  __shared__ Lds L;
  const int tid = threadIdx.x, lane = tid & 63, g = lane >> 4, c = lane & 15;
  const int wave = __builtin_amdgcn_readfirstlane(tid >> 6);
  const int b = blockIdx.x >> 5, h = 4 * (blockIdx.x & 7) + ((blockIdx.x >> 3) & 3), bh = b * 32 + h;
  const bool ffn = h < NFFN;
  unsigned long long pc0;
  asm volatile("s_getpc_b64 %0\nfb_code_begin:" : "=s"(pc0));
  unsigned cpf = 0;
  if (wave == 4) {
    unsigned len;
    asm volatile("s_mov_b32 %0, fb_code_end-fb_code_begin" : "=s"(len));
    cpf = __builtin_amdgcn_raw_buffer_load_b32(mk((const void*)(pc0 & ~63ull), len), lane < 24 ? ((blockIdx.x >> 3) * 24 + lane) * 64 : OOB, 0, 0);
  }
  Masks mk4;
#pragma unroll
  for (int j = 0; j < 4; ++j) mk4.m[j] = (((c & 7) >> 1) == j) ? ((c & 1) ? 0xffff0000u : 0x0000ffffu) : 0u;

  u32x4 k0[EKN];
  unsigned ts0;
  {
    int wv = wave;
    asm volatile("" : "+s"(wv) : "s"(x), "s"(kc), "s"(vc), "s"(wq), "s"(wo), "s"(wg), "s"(wu), "s"(wd), "s"(out), "s"(ws));
    if (wv < 4) {
      const rsrc_t r = mk((const unsigned char*)kc + (size_t)bh * (SK * DH * 2), SK * DH * 2);
      ts0 = now();
#pragma unroll
      for (int j = 0; j < EKN; ++j) k0[j] = __builtin_amdgcn_raw_buffer_load_b128(r, c * 16 + spread(g, 0, 2) + spread(wv, 2, 2), offi(j), AUX_NT);
    }
    __builtin_amdgcn_sched_barrier(0);
  }
  if (wave < 4) {
    const int w = wave;
    const unsigned char* kb = (const unsigned char*)kc + (size_t)bh * (SK * DH * 2);
    const unsigned char* vb = (const unsigned char*)vc + (size_t)bh * (SK * DH * 2);
    const rsrc_t rK = mk(kb, SK * DH * 2), rV = mk(vb, SK * DH * 2);
    const unsigned voff = c * 16 + spread(g, 0, 2) + spread(w, 2, 2);
    unsigned tb = ts0 + T0, tn = tb, bs = tb;
    bool far = false;
    auto pace = [&](int i, int n, bool thr = true) {
      if (i % PSTEP == 0) {
        tb += min(max((int)(bs - tb), -SLEW), SLEW);
        far = (int)(tn - tb - due(n)) > LAGT;
      }
      if (thr && !far) asm volatile("s_waitcnt vmcnt(%0)" ::"n"(KW - 1));
      if (i % PSTEP == 0) {
        const unsigned tgt = tb + due(n);
        if (__builtin_expect((int)(tn - tgt) < 0, 1))
#pragma unroll 1
          for (int k = 0; k < 256 && (int)(now() - tgt) < 8; ++k) __builtin_amdgcn_s_sleep(1);
      }
    };
    auto clock = [&](int i) { if (i % PSTEP == PSTEP - 1) tn = now(); };
    auto rebase = [&]() {
      bs = __builtin_amdgcn_readfirstlane(__hip_atomic_load(&L.base, __ATOMIC_RELAXED, __HIP_MEMORY_SCOPE_WORKGROUP));
    };
    auto report = [&](int n) { __hip_atomic_store(&L.at[w], tn - due(n), __ATOMIC_RELAXED, __HIP_MEMORY_SCOPE_WORKGROUP); };
    u32x4 ring[16], pre[48];
    auto kslot = [&](auto J) -> u32x4& { if constexpr (J.value < 48) return pre[J.value]; else return ring[J.value & 15]; };
    auto kissue = [&](auto J) {
      constexpr int win = J.value < KP ? KWI : KD;
      if constexpr (J.value >= win) touch(kslot(std::integral_constant<int, J.value - win>{}));
      if constexpr (J.value >= KP && (J.value & 7) == 0) rebase();
      pace(J.value, J.value, J.value >= KP);
      kslot(J) = __builtin_amdgcn_raw_buffer_load_b128(rK, voff, offg(J.value >> 4) + offi(J.value & 15), AUX_NT);
      clock(J.value);
      if constexpr ((J.value & 7) == 7 && J.value >= 16) report(J.value);
      __builtin_amdgcn_sched_barrier(0);
    };
    sfor<0, EKN>([&](auto J) { pre[J.value] = k0[J.value]; });
    asm volatile("s_barrier" ::: "memory");
    sfor<EKN, KP>(kissue);
    bar();
    const unsigned tag = L.tag;
    unsigned q[4];
    {
      f32x4 a0 = {0, 0, 0, 0}, a1 = {0, 0, 0, 0};
#pragma unroll
      for (int s = 0; s < 4; ++s) {
        a0 += *(const f32x4*)&L.qpart[s][8 * c];
        a1 += *(const f32x4*)&L.qpart[s][8 * c + 4];
      }
      q[0] = pk_bf(a0[0], a0[1]); q[1] = pk_bf(a0[2], a0[3]); q[2] = pk_bf(a1[0], a1[1]); q[3] = pk_bf(a1[2], a1[3]);
    }
    const bool b3 = c & 8, b2 = c & 4, b1 = c & 2, b0 = c & 1;
    float t[5], m, l;
    auto score = [&](int G, bool first) {
      const float s = rbf(rbf(t[4]) * 0.08838834764831845f);
      L.sc[w][G][lane] = s;
      if (first) { m = s; l = 1.f; }
      else { const float mn = fmaxf(m, s); l = l * fexp(m - mn) + fexp(s - mn); m = mn; }
    };
    sfor<0, 48>([&](auto J) {
      constexpr int lo = J.value == 0 ? KP : sched(J.value - 1, KP), hi = sched(J.value, KP);
      static_assert(hi - J.value <= 41, "register budget");
      sfor<lo, hi>(kissue);
      kstep<J.value & 15>(pre[J.value], q, t, b3, b2, b1, b0);
      if constexpr ((J.value & 15) == 15) score(J.value >> 4, J.value == 15);
      __builtin_amdgcn_sched_barrier(0);
    });
    static_assert(sched(47, KP) == 48 + KD, "backlog must be drained inside the unrolled groups");
    for (int G = 3; G < 16; ++G) {
      const bool last = G == 15;
      const rsrc_t rN = mk(last ? vb : kb, SK * DH * 2);
      const int so = offg(G), sn = last ? 0 : offg(G + 1);
      sfor<0, 16>([&](auto I) {
        constexpr int i = I.value;
        if constexpr ((i & 7) == 0) rebase();
        touch(ring[i]);
        pace(i, G * 16 + i + KD);
        if constexpr (i + KD < 16) ring[i + KD] = __builtin_amdgcn_raw_buffer_load_b128(rK, voff, so + offi(i + KD), AUX_NT);
        else ring[i + KD - 16] = __builtin_amdgcn_raw_buffer_load_b128(rN, voff, sn + offi(i + KD - 16), AUX_NT);
        clock(i);
        if constexpr ((i & 7) == 7) report(G * 16 + i + KD);
        __builtin_amdgcn_sched_barrier(0);
        kstep<i>(ring[i], q, t, b3, b2, b1, b0);
        if constexpr (i == 15) score(G, false);
        __builtin_amdgcn_sched_barrier(0);
      });
    }
    static_assert(KD + KX < 16, "ring");
    auto vslot = [&](auto J) -> u32x4& { return ring[J.value & 15]; };
    auto vissue = [&](auto J) {
      touch(vslot(std::integral_constant<int, J.value - KD>{}));
      pace(J.value, 256 + J.value);
      vslot(J) = __builtin_amdgcn_raw_buffer_load_b128(rV, voff, offg(J.value >> 4) + offi(J.value & 15), AUX_NT);
      clock(J.value);
      __builtin_amdgcn_sched_barrier(0);
    };
    float snext = L.sc[w][0][lane];
    vissue(std::integral_constant<int, KD>{});
    const float mw = wave_max(m);
    const float lw = wave_sum(l * fexp(m - mw));
    if (lane == 0) {
      L.ml[w][0] = mw; L.ml[w][1] = lw;
      asm volatile("s_waitcnt lgkmcnt(0)" ::: "memory");
      __hip_atomic_fetch_add(&L.cntm, 1u, __ATOMIC_RELAXED, __HIP_MEMORY_SCOPE_WORKGROUP);
    }
    __builtin_amdgcn_sched_barrier(0);
    sfor<KD + 1, KD + KX>(vissue);
#pragma unroll 1
    for (int spin = 0; spin < SPIN_CAP && __hip_atomic_load(&L.cntm, __ATOMIC_RELAXED, __HIP_MEMORY_SCOPE_WORKGROUP) < 4u; ++spin) {}
    asm volatile("" ::: "memory");
    float M, inv;
    {
      const f32x4 a = *(const f32x4*)&L.ml[0][0], e = *(const f32x4*)&L.ml[2][0];
      M = fmaxf(fmaxf(a[0], a[2]), fmaxf(e[0], e[2]));
      const float tot = a[1] * fexp(a[0] - M) + a[3] * fexp(a[2] - M) + e[1] * fexp(e[0] - M) + e[3] * fexp(e[2] - M);
      inv = 1.f / tot;
    }
    auto prob = [&](float s) { const float p = fexp(s - M) * inv; return pk_bf(p, p); };
    unsigned pp = prob(snext), pn;
    f32x4 acc0 = {0, 0, 0, 0}, acc1 = {0, 0, 0, 0};
    auto vstep = [&](auto I, const u32x4& v) {
      constexpr int i = I.value;
      const u32x4 B = bsel<rev4(i)>(pp, mk4);
      if constexpr (i & 1) acc1 = mm(v, B, acc1); else acc0 = mm(v, B, acc0);
    };
    sfor<0, 16>([&](auto J) {
      constexpr int lo = J.value == 0 ? KD + KX : sched(J.value - 1, KD + KX), hi = sched(J.value, KD + KX);
      sfor<lo, hi>(vissue);
      if constexpr (J.value == 0) snext = L.sc[w][1][lane];
      if constexpr (J.value == 8) pn = prob(snext);
      vstep(J, vslot(J));
      __builtin_amdgcn_sched_barrier(0);
    });
    static_assert(sched(15, KD + KX) == 16 + KD, "V backlog must be drained inside group 0");
    pp = pn;
    for (int G = 1; G < 15; ++G) {
      const int so = offg(G), sn = offg(G + 1);
      sfor<0, 16>([&](auto I) {
        constexpr int i = I.value;
        if constexpr ((i & 7) == 0) rebase();
        touch(ring[i]);
        pace(i, 256 + G * 16 + i + KD);
        if constexpr (i + KD < 16) ring[i + KD] = __builtin_amdgcn_raw_buffer_load_b128(rV, voff, so + offi(i + KD), AUX_NT);
        else ring[i + KD - 16] = __builtin_amdgcn_raw_buffer_load_b128(rV, voff, sn + offi(i + KD - 16), AUX_NT);
        clock(i);
        if constexpr ((i & 7) == 7) report(256 + G * 16 + i + KD);
        __builtin_amdgcn_sched_barrier(0);
        if constexpr (i == 0) snext = L.sc[w][G + 1][lane];
        if constexpr (i == 8) pn = prob(snext);
        vstep(I, ring[i]);
        __builtin_amdgcn_sched_barrier(0);
      });
      pp = pn;
    }
    const unsigned short* const gp = (c < 8 ? wg : wu) + (size_t)(160 * w + 8 * g) * F + 64 * (ffn ? h : 0) + 8 * (c & 7);
    u32x4 gf[5], gu[40];
    sfor<0, 16>([&](auto I) {
      constexpr int i = I.value;
      touch(ring[i]);
      if constexpr (i + KD < 16) {
        pace(i, 496 + i + KD);
        ring[i + KD] = __builtin_amdgcn_raw_buffer_load_b128(rV, voff, offg(15) + offi(i + KD), AUX_NT);
        clock(i);
      }
      if constexpr (i == GE) {
#pragma unroll
        for (int k = 0; k < 5; ++k) { const int f = ffn ? 5 * b + k : 0; gf[k] = *(const u32x4*)(gp + (32 * (f >> 3) + (f & 7)) * F); }
      }
      __builtin_amdgcn_sched_barrier(0);
      vstep(I, ring[i]);
      __builtin_amdgcn_sched_barrier(0);
    });
    acc0 += acc1;
    if (c < 8) *(f32x4*)&L.opart[w][(g * 8 + c) * 4] = acc0;
    bar();
    auto guload = [&](auto K, unsigned t) {
      constexpr int k = K.value;
      wait_until(t);
      gu[k] = *(const u32x4*)(gp + (32 * (k >> 3) + (k & 7)) * F);
      __builtin_amdgcn_sched_barrier(0);
    };
    if (ffn) {
      const unsigned t = now();
      sfor<0, GB>([&](auto K) { guload(K, t + K.value * GP); });
    }
    bar();
    const unsigned ppos = 20 * (tid >> 3) + 4 * ((tid & 7) < 5 ? tid & 7 : 0), poff = (tid >> 3) * 4096 + (tid & 7) * 16;
    auto publish = [&](unsigned base) {
      f32x4 s = *(const f32x4*)&L.part[0][ppos] + *(const f32x4*)&L.part[1][ppos];
      s += *(const f32x4*)&L.part[2][ppos] + *(const f32x4*)&L.part[3][ppos];
      __builtin_amdgcn_raw_buffer_store_b128(seal(s, tag), mk(ws + base + b * 131072, 131072), poff, h * 128, AUX_SC1);
    };
    publish(WS_G1);
    if (!ffn) {
#pragma unroll
      for (int k = 0; k < 5; ++k) touch(gf[k]);
    }
    if (ffn) {
      __builtin_amdgcn_sched_barrier(0);
      const unsigned t = now();
      sfor<GB, 40>([&](auto K) { guload(K, t + (K.value - GB) * GP); });
#pragma unroll
      for (int k = 0; k < 5; ++k) touch(gf[k]);
      sfor<0, GT>([&](auto K) { touch(gu[K.value]); });
      bar();
      unsigned hv[3];
#pragma unroll
      for (int r = 0; r < 3; ++r) hv[r] = L.hn[160 * w + 64 * r + 4 * c + g];
      f32x4 a0 = {0, 0, 0, 0}, a1 = {0, 0, 0, 0};
      u32x4 Bn = bsel<0>(hv[0], mk4);
      sfor<0, 40>([&](auto K) {
        constexpr int k = K.value;
        const u32x4 B = Bn;
        if constexpr (k < 39) Bn = bsel<(k + 1) & 15>(hv[(k + 1) >> 4], mk4);
        __builtin_amdgcn_sched_barrier(0);
        if constexpr (k & 1) a1 = mm(gu[k], B, a1); else a0 = mm(gu[k], B, a0);
        __builtin_amdgcn_sched_barrier(0);
      });
      a0 += a1;
      if (c < 8) *(f32x4*)&L.gpart[w][(g * 8 + c) * 4] = a0;
      bar();
      bar();
      publish(WS_G3);
      asm volatile("fb_code_end:");
    }
  } else {
    const int w = wave - 4;
    const rsrc_t rE = mk(ws + b * 256, 256);
    const unsigned tstart = now();
    const rsrc_t rT = mk(ws + WS_QT, 256 * 128);
    const int pb = 4 * (g & 1) + 2 * (g >> 1) + (c & 1);
    unsigned tagp, tagq, tag;
    {
      const int kq = b >> 1, half = b & 1, j = lane & 31, sb = 2 * w + (lane >> 5);
      const rsrc_t rX = mk(x, 8 * D * 2);
      u32x2 xs[5];
#pragma unroll
      for (int k = 0; k < 5; ++k) {
        const int e = 160 * kq + 128 * k + 4 * j;
        xs[k] = __builtin_amdgcn_raw_buffer_load_b64(rX, sb * (D * 2) + 2 * (e < D ? e : e - D), 0, 0);
      }
      const rsrc_t rQ = mk(wq + (size_t)(160 * kq + 40 * w) * HD + h * DH + 64 * half, 40 * HD * 2);
      u32x4 wf[5];
#pragma unroll
      for (int k = 0; k < 5; ++k) wf[k] = __builtin_amdgcn_raw_buffer_load_b128(rQ, (lane >> 3) * (HD * 2) + (lane & 7) * 16, k * 8 * HD * 2, 0);
      tagp = __builtin_amdgcn_raw_buffer_load_b32(rT, (pb * 32 + h) * 128, 0, AUX_SC1);
      tagq = __builtin_amdgcn_raw_buffer_load_b32(rT, 0, bh * 128, AUX_SC1);
      tag = __builtin_amdgcn_raw_buffer_load_b32(rE, 0, 0, AUX_SC1);
      if (tid == 256) { L.cnt = 0; L.cntm = 0; L.base = tstart + T0; }
      if (w == 3 && lane < 4) L.at[lane] = tstart + T0;
      __builtin_amdgcn_sched_barrier(0);
      bar();
      asm volatile("" : "+v"(xs[0]), "+v"(xs[1]), "+v"(xs[2]), "+v"(xs[3]), "+v"(xs[4]));
      asm volatile("" ::"v"(cpf));
      float s = 0.f;
#pragma unroll
      for (int k = 0; k < 5; ++k) s += (bf_lo(xs[k][0]) + bf_hi(xs[k][0])) + (bf_lo(xs[k][1]) + bf_hi(xs[k][1]));
      const float mean = half_sum(s) * (1.f / D);
      float v = 0.f, v2 = 0.f;
#pragma unroll
      for (int k = 0; k < 10; ++k) { const unsigned u = xs[k >> 1][k & 1]; const float a = bf_lo(u) - mean, e = bf_hi(u) - mean; v += a * a; v2 += e * e; }
      const float var = half_sum(v + v2) * (1.f / D);
      const float mb = rbf(mean), rden = __builtin_amdgcn_rcpf(rbf(__builtin_amdgcn_sqrtf(rbf(rbf(var) + 1e-5f))));
      unsigned char* const xn = (unsigned char*)L.down + 160 * 128;
#pragma unroll
      for (int k = 0; k < 2; ++k) {
        const unsigned u0 = xs[k][0], u1 = xs[k][1];
        const unsigned d0 = pk_bf(bf_lo(u0) - mb, bf_hi(u0) - mb), d1 = pk_bf(bf_lo(u1) - mb, bf_hi(u1) - mb);
        const u32x2 n = {pk_bf(bf_lo(d0) * rden, bf_hi(d0) * rden), pk_bf(bf_lo(d1) * rden, bf_hi(d1) * rden)};
        if (k == 0 || j < 8) *(u32x2*)(xn + sb * 336 + 256 * k + 8 * j) = n;
      }
#pragma unroll
      for (int k = 0; k < 5; ++k) {
        const int r = 40 * w + 8 * k + (lane >> 3);
        *(u32x4*)((unsigned char*)L.down + r * 128 + (((lane & 7) ^ ((r & 2) | ((r & 8) >> 1))) << 4)) = wf[k];
      }
      asm volatile("s_waitcnt lgkmcnt(0)" ::: "memory");
      if (lane == 0) __hip_atomic_fetch_add(&L.cnt, 1u, __ATOMIC_RELAXED, __HIP_MEMORY_SCOPE_WORKGROUP);
#pragma unroll 1
      for (int spin = 0; spin < SPIN_CAP && __hip_atomic_load(&L.cnt, __ATOMIC_RELAXED, __HIP_MEMORY_SCOPE_WORKGROUP) < 4u; ++spin) {}
      asm volatile("" ::: "memory");
      f32x4 a0 = {0, 0, 0, 0};
      {
        const int r = 8 * g + (c >> 2);
        const unsigned char* bp = (const unsigned char*)L.down + r * 128 + (((2 * w + ((c >> 1) & 1)) ^ ((r & 2) | ((r & 8) >> 1))) << 4) + (c & 1) * 8;
        const unsigned char* ap = xn + (c & 7) * 336 + g * 16;
#pragma unroll
        for (int k = 0; k < 5; ++k) {
          const u32x2 lo = trd(bp + k * 4096), hi = trd(bp + k * 4096 + 512);
          const u32x4 B = {lo[0], lo[1], hi[0], hi[1]};
          a0 = mm(*(const u32x4*)(ap + 64 * k), B, a0);
        }
      }
      tagp += 1; tagq += 1; tag += 1;
      const float v0 = g < 2 ? a0[0] : a0[2], v1 = g < 2 ? a0[1] : a0[3];
      const float rc = dppf<0xB1>((c & 1) ? v0 : v1);
      const u32x4 gr = {__float_as_uint((c & 1) ? rc : v0), tagp, __float_as_uint((c & 1) ? v1 : rc), tagp};
      __builtin_amdgcn_raw_buffer_store_b128(gr, mk(ws + WS_Q + h * 4096 + kq * 1024 + half * 512 + w * 128, 8 * 32 * 4096),
                                             pb * (32 * 4096) + (c >> 1) * 16, 0, AUX_SC1);
      if (w == 0) {
        const u32x2 st2 = {tstart, tag};
        __builtin_amdgcn_raw_buffer_store_b64(st2, mk(ws + WS_ST + b * 256, 256), lane == 0 ? h * 8 : OOB, 0, AUX_SC1);
      }
      if (tid == 256) L.tag = tag;
      wait_until(tstart + QT);
      const rsrc_t rM = mk(ws + WS_Q + bh * 4096 + w * 1024, 1024);
      u32x4 u;
#pragma unroll 1
      for (int spin = 0; spin < SPIN_CAP; ++spin) {
        asm volatile("" ::: "memory");
        u = __builtin_amdgcn_raw_buffer_load_b128(rM, lane * 16, 0, AUX_SC1);
        if (__all(u[1] == tagq && u[3] == tagq)) break;
        __builtin_amdgcn_s_sleep(4);
      }
      const f32x2 qq = {__uint_as_float(u[0]), __uint_as_float(u[2])};
      *(f32x2*)&L.qpart[w][2 * lane] = qq;
    }
    bar();
    if (w == 0) __builtin_amdgcn_raw_buffer_store_b32(tagq, rT, lane == 0 ? 0 : OOB, bh * 128, AUX_SC1);
    if (w == 3) {
      const rsrc_t r = mk(ws + WS_ST + b * 256, 256);
#pragma unroll 1
      for (int spin = 0; spin < 64; ++spin) {
        asm volatile("" ::: "memory");
        const u32x2 u = __builtin_amdgcn_raw_buffer_load_b64(r, (lane & 31) * 8, 0, AUX_SC1);
        if (__all(u[1] == tag)) {
          const float d = half_sum((float)(int)(u[0] - tstart)) * (1.f / 32);
          if (lane == 0) __hip_atomic_store(&L.base, tstart + T0 + (int)d, __ATOMIC_RELAXED, __HIP_MEMORY_SCOPE_WORKGROUP);
          break;
        }
        __builtin_amdgcn_s_sleep(16);
      }
    }
    const unsigned wvoff = (4 * w + g) * (D * 2) + c * 16;
    u32x4 wf[40];
    {
      const rsrc_t rO = mk(wo + (size_t)(128 * h) * D, 128 * D * 2);
      const rsrc_t rD = mk(wd + (size_t)(64 * (ffn ? h : 0)) * D, ffn ? 64 * D * 2 : 0);
      unsigned ref = __builtin_amdgcn_readfirstlane(__hip_atomic_load(&L.base, __ATOMIC_RELAXED, __HIP_MEMORY_SCOPE_WORKGROUP));
      const unsigned tw = ref + W0;
      auto nap = [&](unsigned t) {
#pragma unroll 1
        for (int n = 0; n < 1024 && (int)(now() - t) < 0; ++n) __builtin_amdgcn_s_sleep(4);
      };
      sfor<0, 12>([&](auto K) {
        constexpr int k = K.value;
        if (k % CE == 0 && w == 3) {
          nap(tw + k * LP);
          const u32x4 a = *(volatile u32x4*)L.at;
          const u32x2 pr = {a[0] + ((int)(a[1] - a[0] + a[2] - a[0] + a[3] - a[0]) >> 2), tag};
          __builtin_amdgcn_raw_buffer_store_b64(pr, mk(ws + WS_PR + k * 2048 + b * 256, 256), lane == 0 ? h * 8 : OOB, 0, AUX_SC1);
          nap(tw + k * LP + LR);
          const u32x2 u = __builtin_amdgcn_raw_buffer_load_b64(mk(ws + WS_PR + k * 2048 + b * 256, 256), (lane & 31) * 8, 0, AUX_SC1);
          const bool ok = u[1] == tag;
          const float n = half_sum(ok ? 1.f : 0.f), d = half_sum(ok ? (float)(int)(u[0] - ref) : 0.f);
          int sh = n >= 8.f ? (int)(d * __builtin_amdgcn_rcpf(n)) - (k ? DLT : DLT0) : 0;
          sh = min(max(sh, 0), 400);
          ref += __builtin_amdgcn_readfirstlane(sh);
          __hip_atomic_store(&L.base, ref, __ATOMIC_RELAXED, __HIP_MEMORY_SCOPE_WORKGROUP);
        }
        if (k == 1 && w == 2) {
          typedef const __attribute__((address_space(4))) unsigned* cptr;
          const int j = blockIdx.x >> 3;
          nap(tw + k * LP + h * LS);
          const unsigned z0 = *(cptr)(vc + (size_t)bh * (SK * DH)), z1 = *(cptr)(wg + j * (D * F / 32)), z2 = *(cptr)(wu + j * (D * F / 32));
          const unsigned z3 = *(cptr)(wg + (1 << 20) + (j & 7) * 7168), z4 = *(cptr)(wu + (1 << 20) + (j & 7) * 7168), z5 = *(cptr)(out + b * D);
          asm volatile("" ::"s"(z0), "s"(z1), "s"(z2), "s"(z3), "s"(z4), "s"(z5));
        }
        if (w < 2 && (k >= 4 || ffn)) {
          typedef const __attribute__((address_space(4))) unsigned* cptr;
          const unsigned short* src = k < 4 ? wd + (size_t)(64 * h + 16 * k) * D : wo + (size_t)(128 * h + 16 * (k - 4)) * D;
          const cptr p = (cptr)(src + (2 * b + w) * D);
          nap(tw + k * LP + LO - SD + h * LS);
          unsigned z[20];
#pragma unroll
          for (int j = 0; j < 20; ++j) z[j] = p[j * 16];
#pragma unroll
          for (int j = 0; j < 20; ++j) asm volatile("" ::"s"(z[j]));
        }
        const unsigned tl = tw + k * LP + LO + h * LS + (3 - w) * LW;
        nap(tl);
        if constexpr (k < 4) {
          u32x4 t[5];
#pragma unroll
          for (int j = 0; j < 5; ++j) {
            if (j) nap(tl + j * LJ);
            t[j] = __builtin_amdgcn_raw_buffer_load_b128(rD, wvoff, k * 16 * D * 2 + j * 256, 0);
            __builtin_amdgcn_sched_barrier(0);
          }
#pragma unroll
          for (int j = 0; j < 5; ++j) L.down[w][5 * k + j][lane] = t[j];
        } else {
#pragma unroll
          for (int j = 0; j < 5; ++j) {
            if (j) nap(tl + j * LJ);
            wf[5 * (k - 4) + j] = __builtin_amdgcn_raw_buffer_load_b128(rO, wvoff, (k - 4) * 16 * D * 2 + j * 256, 0);
            __builtin_amdgcn_sched_barrier(0);
          }
        }
        __builtin_amdgcn_sched_barrier(0);
      });
    }
    const int col = pos2col(20 * h + 4 * ((c & 7) < 5 ? c & 7 : 0) + g);
    const float x0 = bf_lo(x[b * D + col]);
    bar();
    {
      const int oi = 32 * ((c & 7) >> 1) + 16 * (w & 1) + 4 * g + 2 * (c & 1) + (w >> 1);
      float o = L.opart[0][oi];
#pragma unroll
      for (int s = 1; s < 4; ++s) o += L.opart[s][oi];
      const unsigned ov = pk_bf(o, o);
      f32x4 a[5];
#pragma unroll
      for (int t = 0; t < 5; ++t) a[t] = f32x4{0, 0, 0, 0};
      u32x4 Bn = bsel<0>(ov, mk4);
      sfor<0, 8>([&](auto I) {
        const u32x4 B = Bn;
        a[0] = mm(wf[I.value * 5], B, a[0]);
        __builtin_amdgcn_sched_barrier(0);
        if constexpr (I.value < 7) Bn = bsel<I.value + 1>(ov, mk4);
        __builtin_amdgcn_sched_barrier(0);
#pragma unroll
        for (int t = 1; t < 5; ++t) a[t] = mm(wf[I.value * 5 + t], B, a[t]);
        __builtin_amdgcn_sched_barrier(0);
      });
      if (c < 8) {
#pragma unroll
        for (int t = 0; t < 5; ++t) *(f32x4*)&L.part[w][128 * t + (g * 8 + c) * 4] = a[t];
      }
    }
    bar();
    const unsigned t1 = now(), tt = (tag & 0xffffu) * 0x10001u;
    sfor<0, 20>([&](auto K) { wf[K.value] = L.down[w][K.value][lane]; });
    float h0 = 0.f;
    unsigned t3 = 0;
    if (w == 1) {
      const rsrc_t r = mk(ws + WS_G1 + bh * 4096, 4096);
      u32x4 v[4];
      wait_until(t1 + DL1);
#pragma unroll 1
      for (int spin = 0; spin < SPIN_CAP; ++spin) {
        asm volatile("" ::: "memory");
#pragma unroll
        for (int k = 0; k < 4; ++k) v[k] = __builtin_amdgcn_raw_buffer_load_b128(r, lane * 16, k * 1024, AUX_SC1);
        unsigned bad = 0;
#pragma unroll
        for (int k = 0; k < 4; ++k) bad |= seal_of(v[k]) ^ tt;
        if (__all(bad == 0)) break;
        __builtin_amdgcn_s_sleep(1);
      }
      h0 = rbf(x0 + rbf(unseal_sum(v)));
      auto p = __builtin_amdgcn_permlane16_swap(__float_as_uint(h0), __float_as_uint(h0), false, false);
      const u32x2 gr = {__builtin_amdgcn_perm(p[1], p[0], 0x07060302u), tag};
      __builtin_amdgcn_raw_buffer_store_b64(gr, mk(ws + WS_G2 + bh * 128, 128), (lane & 24) == 0 ? c * 16 + (g >> 1) * 8 : OOB, 0, AUX_SC1);
      const unsigned t2 = now();
      if (h == 0) __builtin_amdgcn_raw_buffer_store_b32(tag, rE, lane == 0 ? 0 : OOB, 0, AUX_SC1);
      const rsrc_t r2 = mk(ws + WS_G2 + b * 4096, 4096);
      unsigned po[3];
#pragma unroll
      for (int k = 0; k < 3; ++k) { const unsigned p = 64 * k + lane, j = p / 5; po[k] = p < 160 ? j * 128 + (p - j * 5) * 16 : OOB; }
      wait_until(t2 + DL2);
      wait_until(t1 + DL12);
#pragma unroll 1
      for (int spin = 0; spin < SPIN_CAP; ++spin) {
        asm volatile("" ::: "memory");
#pragma unroll
        for (int k = 0; k < 3; ++k) v[k] = __builtin_amdgcn_raw_buffer_load_b128(r2, po[k], 0, AUX_SC1);
        const bool ok = v[0][1] == tag && v[0][3] == tag && v[1][1] == tag && v[1][3] == tag &&
                        ((v[2][1] == tag && v[2][3] == tag) || lane >= 32);
        if (__all(ok)) break;
        __builtin_amdgcn_s_sleep(1);
      }
      t3 = now() + DL23;
      if (ffn) {
        float e[12];
#pragma unroll
        for (int k = 0; k < 3; ++k) {
          e[4 * k] = bf_lo(v[k][0]); e[4 * k + 1] = bf_hi(v[k][0]); e[4 * k + 2] = bf_lo(v[k][2]); e[4 * k + 3] = bf_hi(v[k][2]);
        }
        const float p = rdl(e[0], 0);
        float s0 = 0.f, s1 = 0.f, q0 = 0.f, q1 = 0.f;
#pragma unroll
        for (int k = 0; k < 8; ++k) { const float a = e[k] - p; s0 += a; q0 += a * a; }
#pragma unroll
        for (int k = 8; k < 12; ++k) { const float a = e[k] - p; s1 += a; q1 += a * a; }
        const float s = wave_sum(lane < 32 ? s0 + s1 : s0) * (1.f / D), q = wave_sum(lane < 32 ? q0 + q1 : q0) * (1.f / D);
        const float mean = p + s, var = fmaxf(q - s * s, 0.f);
        const float mb = rbf(mean), rden = __builtin_amdgcn_rcpf(rbf(__builtin_amdgcn_sqrtf(rbf(rbf(var) + 1e-5f))));
#pragma unroll
        for (int k = 0; k < 3; ++k) {
          u32x4 o;
#pragma unroll
          for (int j = 0; j < 4; ++j) { const float n = rbf(e[4 * k + j] - mb) * rden; o[j] = pk_bf(n, n); }
          if (k < 2 || lane < 32) *(u32x4*)&L.hn[4 * (64 * k + lane)] = o;
        }
      }
    }
    if (ffn) {
      bar();
      bar();
      const int u = 32 * ((c & 3) >> 1) + 16 * (w & 1) + 4 * g + 2 * (c & 1) + (w >> 1);
      float sg = L.gpart[0][u], su = L.gpart[0][64 + u];
#pragma unroll
      for (int s = 1; s < 4; ++s) { sg += L.gpart[s][u]; su += L.gpart[s][64 + u]; }
      sg = rbf(sg);
      const float act = rbf(sg * __builtin_amdgcn_rcpf(1.f + fexp(-sg))) * rbf(su);
      const unsigned av = pk_bf(act, act);
      f32x4 a[5];
#pragma unroll
      for (int t = 0; t < 5; ++t) a[t] = f32x4{0, 0, 0, 0};
      u32x4 Bn = bsel<0>(av, mk4);
      sfor<0, 4>([&](auto I) {
        const u32x4 B = Bn;
        a[0] = mm(wf[I.value * 5], B, a[0]);
        __builtin_amdgcn_sched_barrier(0);
        if constexpr (I.value < 3) Bn = bsel<I.value + 1>(av, mk4);
        __builtin_amdgcn_sched_barrier(0);
#pragma unroll
        for (int t = 1; t < 5; ++t) a[t] = mm(wf[I.value * 5 + t], B, a[t]);
        __builtin_amdgcn_sched_barrier(0);
      });
      if (c < 8) {
#pragma unroll
        for (int t = 0; t < 5; ++t) *(f32x4*)&L.part[w][128 * t + (g * 8 + c) * 4] = a[t];
      }
      bar();
      t3 = now() + DL3;
    }
    if (w == 1) {
      const rsrc_t r = mk(ws + WS_G3 + bh * 4096, NFFN * 128);
      u32x4 v[4];
      wait_until(t3);
#pragma unroll 1
      for (int spin = 0; spin < SPIN_CAP; ++spin) {
        asm volatile("" ::: "memory");
#pragma unroll
        for (int k = 0; k < 4; ++k) v[k] = __builtin_amdgcn_raw_buffer_load_b128(r, lane * 16 + k * 1024, 0, AUX_SC1);
        const unsigned bad = (seal_of(v[0]) ^ tt) | (seal_of(v[1]) ^ tt) | (seal_of(v[2]) ^ tt) | (lane < 24 ? seal_of(v[3]) ^ tt : 0u);
        if (__all(bad == 0)) break;
        __builtin_amdgcn_s_sleep(1);
      }
      const unsigned o = pk_bf(h0 + rbf(unseal_sum(v)), 0.f);
      if (c < 5) out[b * D + col] = (unsigned short)o;
    }
  }
}

extern "C" void kernel_launch(void* const* d_in, const int* in_sizes, int n_in, void* d_out, int out_size, void* d_ws,
                              size_t ws_size, hipStream_t stream) {
  fused_block<<<256, 512, 0, stream>>>((const unsigned short*)d_in[0], (const unsigned short*)d_in[1], (const unsigned short*)d_in[2],
                                       (const unsigned short*)d_in[3], (const unsigned short*)d_in[4], (const unsigned short*)d_in[6],
                                       (const unsigned short*)d_in[7], (const unsigned short*)d_in[8], (unsigned short*)d_out,
                                       (unsigned char*)d_ws);
}
```
